```python
import torch

def _softmax(x, axis=-1):
    return torch.softmax(x, dim=axis)
def _gelu(x):
    return torch.nn.functional.gelu(x, approximate="tanh")
def _silu(x):
    return torch.nn.functional.silu(x)
def _sigmoid(x):
    return torch.sigmoid(x)
def _layernorm(x, axis=-1, eps=1e-5):
    m = x.mean(dim=axis, keepdim=True)
    v = x.var(dim=axis, keepdim=True, unbiased=False)
    return (x - m) / torch.sqrt(v + eps)
def _scatter_add(tgt, idx, src, axis=0):
    return tgt.clone().index_add_(axis, idx, src)
def _segment_sum(src, seg, n):
    out = torch.zeros((n,) + src.shape[1:], dtype=src.dtype)
    return out.index_add_(0, seg, src)
def _pad(x, pw):
    flat = tuple(v for pair in reversed(pw) for v in pair)
    return torch.nn.functional.pad(x, flat)

kernel_name = 'recurrent_scan__ssm_conv_s4__mixer_channel_mlp__prompt'

def setup_inputs():

    _w = (lambda a: (lambda t: t.to(torch.bfloat16) if t.is_floating_point() else t)(torch.as_tensor(a)))
    def _A_setup():
        B, L, D, N = 1, 2048, 640, 128
        A_re = -(lambda _x: torch.exp(torch.as_tensor(_x)))((lambda *s: torch.randn(*s, dtype=torch.bfloat16) * 3.2)(D, N))
        A_im = torch.pi * (lambda *s: torch.randn(*s, dtype=torch.bfloat16) * 0.05)(D, N)
        B_re = (lambda *s: torch.randn(*s, dtype=torch.bfloat16) * 0.2)(D, N)
        B_im = (lambda *s: torch.randn(*s, dtype=torch.bfloat16) * 0.2)(D, N)
        C_re = (lambda *s: torch.randn(*s, dtype=torch.bfloat16) * 0.2)(D, N)
        C_im = (lambda *s: torch.randn(*s, dtype=torch.bfloat16) * 0.2)(D, N)
        log_dt = (lambda *s: torch.randn(*s, dtype=torch.bfloat16) * 0.05)(D) * 0.5 - 4.0
        Dskip = (lambda *s: torch.randn(*s, dtype=torch.bfloat16) * 0.05)(D)
        return {
            "u": _w((lambda *s: torch.randn(*s, dtype=torch.bfloat16) * 0.05)(B, L, D)),
            "A_re": _w(A_re),
            "A_im": _w(A_im),
            "B_re": _w(B_re),
            "B_im": _w(B_im),
            "C_re": _w(C_re),
            "C_im": _w(C_im),
            "log_dt": _w(log_dt),
            "Dskip": _w(Dskip),
        }
    _A_in = _A_setup()

    def _B_setup():
        B, S, D, F = 1, 2048, 640, 1728
        return {
            "x": _w((lambda *s: torch.randn(*s, dtype=torch.bfloat16) * 0.05)(B, S, D)),
            "w1": _w((lambda *s: torch.randn(*s, dtype=torch.bfloat16) * 3.2)(D, F) * (D ** -0.5)),
            "b1": _w((lambda *s: torch.randn(*s, dtype=torch.bfloat16) * 0.8)(F)),
            "w2": _w((lambda *s: torch.randn(*s, dtype=torch.bfloat16) * 0.05)(F, D) * (F ** -0.5)),
            "b2": _w((lambda *s: torch.randn(*s, dtype=torch.bfloat16) * 0.05)(D)),
        }
    _B_in = _B_setup()

    return {
        "A_u": _A_in["u"],
        "A_A_re": _A_in["A_re"],
        "A_A_im": _A_in["A_im"],
        "A_B_re": _A_in["B_re"],
        "A_B_im": _A_in["B_im"],
        "A_C_re": _A_in["C_re"],
        "A_C_im": _A_in["C_im"],
        "A_log_dt": _A_in["log_dt"],
        "A_Dskip": _A_in["Dskip"],
        "B_x": _B_in["x"],
        "B_w1": _B_in["w1"],
        "B_b1": _B_in["b1"],
        "B_w2": _B_in["w2"],
        "B_b2": _B_in["b2"],
    }

def reference(A_u, A_A_re, A_A_im, A_B_re, A_B_im, A_C_re, A_C_im, A_log_dt, A_Dskip, B_x, B_w1, B_b1, B_w2, B_b2):
    def _A_ref(u, A_re, A_im, B_re, B_im, C_re, C_im, log_dt, Dskip):
        Bsz, L, D = u.shape
        dt = torch.exp(log_dt)[:, None]
        A = A_re + 1j * A_im
        Bc = B_re + 1j * B_im
        Cc = C_re + 1j * C_im
        logdA = dt * A
        dB = dt * Bc
        l = (lambda x, dt: x.to(dt))(torch.arange(L), A_re.dtype)
        P = torch.exp(logdA[:, :, None] * l[None, None, :])
        K = torch.einsum("dn,dnl,dn->dl", Cc, P, dB).real
        L2 = 2 * L
        Kp = _pad(K, ((0, 0), (0, L)))
        up = (lambda x, *d: x.permute(*d))(u, 0, 2, 1)
        up = _pad(up, ((0, 0), (0, 0), (0, L)))
        Kf = (lambda x, axes=(-2,-1): torch.fft.rfft2(x.float(), dim=axes))(Kp[:, None, :], axes=(-2, -1))
        Uf = (lambda x, axes=(-2,-1): torch.fft.rfft2(x.float(), dim=axes))(up[:, :, None, :], axes=(-2, -1))
        Yf = Uf * Kf[None, :, :, :]
        y = (lambda x, s, axes=(-2,-1): torch.fft.irfft2(x, s=s, dim=axes).to(torch.bfloat16))(Yf, (1, L2), axes=(-2, -1))[:, :, 0, :L]
        y = (lambda x, *d: x.permute(*d))(y, 0, 2, 1) + u * Dskip[None, None, :]
        return y

    def _B_ref(x, w1, b1, w2, b2):
        y = _layernorm(x, axis=-1)
        h = _gelu(torch.einsum("bsd,df->bsf", y, w1) + b1)
        return torch.einsum("bsf,fd->bsd", h, w2) + b2

    x = A_u
    xn = _layernorm(x, axis=-1)
    s = _A_ref(xn, A_A_re, A_A_im, A_B_re, A_B_im, A_C_re, A_C_im, A_log_dt, A_Dskip)
    g = _silu(_B_ref(xn, B_w1, B_b1, B_w2, B_b2))
    return x + s * g
```

```cpp
#include <hip/hip_runtime.h>
#include <stdint.h>

namespace {

template <int V> struct IC { static constexpr int value = V; };

typedef unsigned short u16;
typedef __attribute__((ext_vector_type(8))) __bf16 bf16x8;
typedef __attribute__((ext_vector_type(2))) __bf16 bf16x2;
typedef __attribute__((ext_vector_type(4))) short s16x4;
typedef __attribute__((ext_vector_type(2))) float f32x2;
typedef __attribute__((ext_vector_type(4))) float f32x4;
typedef __attribute__((ext_vector_type(16))) float f32x16;
typedef __attribute__((ext_vector_type(2))) unsigned u32x2;
typedef __attribute__((ext_vector_type(4))) unsigned u32x4;
#define LDSP __attribute__((address_space(3)))
typedef LDSP char* lptr;
typedef __amdgpu_buffer_rsrc_t rsrc_t;

constexpr int L = 2048, D = 640, NS = 128, F = 1728;
constexpr int KS2 = F / 32;
constexpr int OFF_EPOCH = 0, OFF_ALL = 4096,
              OFF_AB = 16384, OFF_CS = 32768, CS_STRIDE = 1792 * 4,
              OFF_FH = 163840,
              OFF_FS = OFF_FH + 32768,
              OFF_FLN = OFF_FS + 2 * D * 128;
constexpr int OFF_H   = 524288;
constexpr int OFF_W2T = OFF_H + 8 * 4 * KS2 * 4096;
constexpr int OFF_XT  = OFF_W2T + 8 * KS2 * 5120;
constexpr int OFF_SP  = OFF_XT + D * L * 2;
constexpr int WS_END  = OFF_SP + D * L * 2;
constexpr int OFF_DBG = WS_END;
constexpr int KG_ROW = 1040, KG_CH = 30 * KG_ROW, L_KG = 98304;
constexpr int L_STG = 65536, STG_ROW = 1344, L_W2S = L_STG + 8 * STG_ROW;
constexpr int KIMG_SZ = 4128, UIMG_SZ = 4352, IMG_PAIR = 2 * KIMG_SZ + 2 * UIMG_SZ;
constexpr int L_IMG = 131072, L_SS = L_IMG + IMG_PAIR, L_KG1 = 2 * IMG_PAIR;
constexpr int L_AB = L_KG + 2 * KG_CH, L_CS = L_AB + 1024, L_B1 = L_CS + 512, L_AB8 = L_B1 + 512, L_FLG = L_AB8 + 64,
              L_DUMMY = L_FLG + 80, LDS_SZ = L_DUMMY + 256;
constexpr int F_RD2 = L_FLG + 64, F_S2F = L_FLG, F_LNC = L_FLG + 4, F_RDY = L_FLG + 8, F_HRDY = L_FLG + 12, F_W2OK = L_FLG + 16, F_PASS = L_FLG + 20, F_PQ = L_FLG + 24, F_AX = L_FLG + 28, F_GP = L_FLG + 32, F_WF = L_FLG + 48;
constexpr int W2L_WAVE = 6 * 5120;
static_assert(LDS_SZ <= 163840 && L_SS + 10240 <= L_AB, "LDS map");
constexpr int SC1 = 16;
constexpr int SPIN_MAX = 1 << 17;

#define PF2 1
#define PF3 0
#define ROT3 1
#define ROT6 0
#define NOW2T 0
#define HPA 2
#define HPB 5
#define HDEP 4
#define MAP2 1
#define LSLEEP 2
#define NSLOW 3
#define THR 8
#define STAMP(k) do {} while (0)
#define TR(k) do {} while (0)

__device__ __forceinline__ float bflo(unsigned p) { return __uint_as_float(p << 16); }
__device__ __forceinline__ float bfhi(unsigned p) { return __uint_as_float(p & 0xffff0000u); }
__device__ __forceinline__ unsigned pk2(float a, float b) {
  f32x2 v = {a, b};
  return __builtin_bit_cast(unsigned, __builtin_convertvector(v, bf16x2));
}
__device__ __forceinline__ float rbf(float a) { return bflo(pk2(a, a)); }
__device__ __forceinline__ void glds16(const void* sbase, unsigned voff, unsigned lds_base) {
  unsigned sv;
  asm volatile("s_mov_b32 %0, m0\n\ts_mov_b32 m0, %3\n\ts_nop 0\n\tglobal_load_lds_dwordx4 %1, %2\n\ts_mov_b32 m0, %0"
               : "=&s"(sv) : "v"(voff), "s"(sbase), "s"(lds_base) : "memory");
}
__device__ __forceinline__ void glds16nt(const void* sbase, unsigned voff, unsigned lds_base) {
  unsigned sv;
  asm volatile("s_mov_b32 %0, m0\n\ts_mov_b32 m0, %3\n\ts_nop 0\n\tglobal_load_lds_dwordx4 %1, %2 nt\n\ts_mov_b32 m0, %0"
               : "=&s"(sv) : "v"(voff), "s"(sbase), "s"(lds_base) : "memory");
}
__device__ __forceinline__ unsigned ldnt(const unsigned* p) { return __builtin_nontemporal_load(p); }
__device__ __forceinline__ void glds16x8(const void* sbase, const unsigned (&v)[8], unsigned lds_base) {
  unsigned sv;
#define G8(i) "s_nop 0\n\tglobal_load_lds_dwordx4 %" #i ", %9\n\ts_add_u32 m0, m0, 0x400\n\t"
  asm volatile("s_mov_b32 %0, m0\n\ts_mov_b32 m0, %10\n\t" G8(1) G8(2) G8(3) G8(4) G8(5) G8(6) G8(7) G8(8) "s_mov_b32 m0, %0"
               : "=&s"(sv)
               : "v"(v[0]), "v"(v[1]), "v"(v[2]), "v"(v[3]), "v"(v[4]), "v"(v[5]), "v"(v[6]), "v"(v[7]), "s"(sbase), "s"(lds_base)
               : "memory", "scc");
#undef G8
}
__device__ __forceinline__ void glds5k(const void* sbase, unsigned voff, unsigned lds_base) {
  unsigned sv;
  asm volatile("s_mov_b32 %0, m0\n\ts_mov_b32 m0, %3\n\ts_nop 0\n\t"
               "global_load_lds_dwordx4 %1, %2 offset:-2048 sc1\n\tglobal_load_lds_dwordx4 %1, %2 offset:-1024 sc1\n\t"
               "global_load_lds_dwordx4 %1, %2 sc1\n\tglobal_load_lds_dwordx4 %1, %2 offset:1024 sc1\n\t"
               "global_load_lds_dwordx4 %1, %2 offset:2048 sc1\n\ts_mov_b32 m0, %0"
               : "=&s"(sv) : "v"(voff), "s"((const char*)sbase + 2048), "s"(lds_base + 2048) : "memory");
}
__device__ __forceinline__ void touch(const void* sbase, unsigned voff, unsigned lds_base) {
  unsigned sv;
  asm volatile("s_mov_b32 %0, m0\n\ts_mov_b32 m0, %3\n\ts_nop 0\n\tglobal_load_lds_dword %1, %2 sc1\n\ts_mov_b32 m0, %0"
               : "=&s"(sv) : "v"(voff), "s"(sbase), "s"(lds_base) : "memory");
}
__device__ __forceinline__ u32x2 trd(lptr p) {
  return __builtin_bit_cast(u32x2, __builtin_amdgcn_ds_read_tr16_b64_v4i16((LDSP s16x4*)p));
}
__device__ __forceinline__ u32x4 trd2(lptr p0, lptr p1) {
  u32x2 a = trd(p0), b = trd(p1);
  return (u32x4){a[0], a[1], b[0], b[1]};
}
__device__ __forceinline__ f32x4 mfma16(u32x4 a, u32x4 b, f32x4 c) {
  return __builtin_amdgcn_mfma_f32_16x16x32_bf16(__builtin_bit_cast(bf16x8, a), __builtin_bit_cast(bf16x8, b), c, 0, 0, 0);
}
__device__ __forceinline__ f32x16 mfma32(u32x4 a, u32x4 b, f32x16 c) {
  return __builtin_amdgcn_mfma_f32_32x32x16_bf16(__builtin_bit_cast(bf16x8, a), __builtin_bit_cast(bf16x8, b), c, 0, 0, 0);
}
#define CFENCE() asm volatile("" ::: "memory")
#define WAITCNT(N) __builtin_amdgcn_s_waitcnt(((N) & 15) | (((N) >> 4) << 14) | 0x70)
#define WAIT_BAR(N) do { CFENCE(); WAITCNT(N); __builtin_amdgcn_s_barrier(); CFENCE(); } while (0)
#define LGKM_BAR() WAIT_BAR(63)
#define DRAIN() asm volatile("s_waitcnt vmcnt(0)" ::: "memory")
#define SBAR() __builtin_amdgcn_sched_barrier(0)
#define PIN(x) asm volatile("" : "+v"(x))

template <int CTRL>
__device__ __forceinline__ float dppadd(float v) {
  return v + __int_as_float(__builtin_amdgcn_update_dpp(0, __float_as_int(v), CTRL, 0xf, 0xf, true));
}
__device__ __forceinline__ float row_pair_add(float v) {
  const auto r = __builtin_amdgcn_permlane16_swap(__float_as_uint(v), __float_as_uint(v), false, false);
  return __uint_as_float(r[0]) + __uint_as_float(r[1]);
}
__device__ __forceinline__ void half_sum2(float& a, float& b) {
  a = dppadd<0xB1>(a); b = dppadd<0xB1>(b); a = dppadd<0x4E>(a); b = dppadd<0x4E>(b);
  a = dppadd<0x141>(a); b = dppadd<0x141>(b); a = dppadd<0x140>(a); b = dppadd<0x140>(b);
  a = row_pair_add(a); b = row_pair_add(b);
}
struct c2 { f32x2 r, i; };
__device__ __forceinline__ c2 cmul(c2 a, c2 b) {
  return {__builtin_elementwise_fma(-a.i, b.i, a.r * b.r), __builtin_elementwise_fma(a.i, b.r, a.r * b.i)};
}
__device__ __forceinline__ c2 cexpl(f32x2 a2, f32x2 rev, float l) {
  const f32x2 ea = a2 * l, ta = rev * l;
  const f32x2 e = {__builtin_amdgcn_exp2f(ea[0]), __builtin_amdgcn_exp2f(ea[1])};
  const float t0 = __builtin_amdgcn_fractf(ta[0]), t1 = __builtin_amdgcn_fractf(ta[1]);
  return {(f32x2){__builtin_amdgcn_cosf(t0), __builtin_amdgcn_cosf(t1)} * e,
          (f32x2){__builtin_amdgcn_sinf(t0), __builtin_amdgcn_sinf(t1)} * e};
}
__device__ __forceinline__ unsigned pkv(f32x2 v) { return __builtin_bit_cast(unsigned, __builtin_convertvector(v, bf16x2)); }
__device__ __forceinline__ void st16(u32x4 v, rsrc_t rs, int voff) { __builtin_amdgcn_raw_buffer_store_b128(v, rs, voff, 0, SC1); }
__device__ __forceinline__ void flag_store(rsrc_t rs, int off, unsigned tag) {
  __builtin_amdgcn_raw_buffer_store_b32(tag, rs, off, 0, SC1);
}
template <int SLEEP>
__device__ __forceinline__ void lds_wait(lptr p, unsigned need) {
#pragma unroll 1
  for (int spin = 0; spin < SPIN_MAX; ++spin) {
    if (__all(*(volatile LDSP unsigned*)p >= need)) break;
    __builtin_amdgcn_s_sleep(SLEEP);
  }
  CFENCE();
}
__device__ __forceinline__ void lds_arrive(lptr p, int lane) {
  CFENCE();
  if (lane == 0) __hip_atomic_fetch_add((LDSP unsigned*)p, 1u, __ATOMIC_RELAXED, __HIP_MEMORY_SCOPE_WORKGROUP);
  CFENCE();
}
__device__ __forceinline__ void lds_set(lptr p, unsigned v) {
  asm volatile("s_mov_b64 exec, 1\n\tds_write_b32 %0, %1\n\ts_mov_b64 exec, -1" :: "v"(p), "v"(v) : "memory");
}
template <int SLEEP>
__device__ __forceinline__ void poll_words(rsrc_t rs, int v0, int v1, unsigned tag) {
#pragma unroll 1
  for (int spin = 0; spin < SPIN_MAX; ++spin) {
    const unsigned a = __builtin_amdgcn_raw_buffer_load_b32(rs, v0, 0, SC1);
    const unsigned b = v1 >= 0 ? __builtin_amdgcn_raw_buffer_load_b32(rs, v1, 0, SC1) : tag;
    if (__all(a == tag && b == tag)) break;
    __builtin_amdgcn_s_sleep(SLEEP);
    CFENCE();
  }
  CFENCE();
}
__device__ __forceinline__ float shift_add(float prev, float cur, float z, bool last) {
  const float m = last ? prev : cur;
  return z + __int_as_float(__builtin_amdgcn_update_dpp(0, __float_as_int(m), 0x13C  , 0xf, 0xf, true));
}

struct Carry { float p1, p2, p3; };
template <int N>
__device__ __forceinline__ void conv_post(const f32x4* z, float* y, Carry& c, bool last) {
  float h2[N], h1[N];
#pragma unroll
  for (int r = 0; r < N; ++r) h2[r] = shift_add(r ? z[r - 1][3] : c.p3, z[r][3], z[r][2], last);
#pragma unroll
  for (int r = 0; r < N; ++r) h1[r] = shift_add(r ? h2[r - 1] : c.p2, h2[r], z[r][1], last);
#pragma unroll
  for (int r = 0; r < N; ++r) y[r] = shift_add(r ? h1[r - 1] : c.p1, h1[r], z[r][0], last);
  c.p1 = h1[N - 1]; c.p2 = h2[N - 1]; c.p3 = z[N - 1][3];
}
__device__ __forceinline__ bool vm_idle() {
  unsigned v;
  asm volatile("s_getreg_b32 %0, hwreg(HW_REG_IB_STS)" : "=s"(v));
  return (v & 0x00C0000Fu) == 0;
}
#define SGB(mask, n) __builtin_amdgcn_sched_group_barrier(mask, n, 0)

__global__ __launch_bounds__(512, 2) void fused(const u16* __restrict__ x, const u16* __restrict__ Are, const u16* __restrict__ Aim,
                                                const u16* __restrict__ Bre, const u16* __restrict__ Bim, const u16* __restrict__ Cre,
                                                const u16* __restrict__ Cim, const u16* __restrict__ logdt, const u16* __restrict__ Dsk,
                                                const u16* __restrict__ w1, const u16* __restrict__ b1, const u16* __restrict__ w2,
                                                const u16* __restrict__ b2, char* ws, u16* __restrict__ out) {
  extern __shared__ __attribute__((aligned(16))) char smem[];
  const lptr lds = (lptr)smem;
  const unsigned lds0 = (unsigned)(uintptr_t)lds;
  int lane = threadIdx.x & 63;
  int w = __builtin_amdgcn_readfirstlane(threadIdx.x >> 6);
  int xcd = blockIdx.x & 7, idx = blockIdx.x >> 3;
  const bool aux = idx < 4;
  int i16 = lane & 15, q = lane >> 4, qq = (lane >> 2) & 3, p = lane & 3;
#define FRESH() do { lane = lane_; w = w_; xcd = xcd_; idx = idx_; m = m_; n = n_; ch0 = ch0_; \
    asm volatile("" : "+v"(lane)); lane &= 63; \
    asm volatile("" : "+s"(w), "+s"(xcd), "+s"(idx), "+s"(m), "+s"(n), "+s"(ch0)); \
    i16 = lane & 15; q = lane >> 4; qq = (lane >> 2) & 3; p = lane & 3; } while (0)
  STAMP(0);
  if (w == 7) {
    uint64_t pc; unsigned len;
    asm volatile("s_getpc_b64 %0\n.Lfused_pc:\n\ts_mov_b32 %1, .Lfused_end-.Lfused_pc" : "=s"(pc), "=s"(len));
    const unsigned o = (idx * 16 + i16) * 128;
    if (o < len) touch((const void*)pc, o, lds0 + L_DUMMY);
  }
  if (w == 0 && lane < 5) *(LDSP u32x4*)(lds + L_FLG + lane * 16) = (u32x4){};
  asm volatile("" :: "s"(x), "s"(Are), "s"(Aim), "s"(Bre), "s"(Bim), "s"(Cre), "s"(Cim), "s"(logdt), "s"(Dsk), "s"(w1), "s"(b1),
               "s"(w2), "s"(b2), "s"(ws), "s"(out));
  const rsrc_t rws = __builtin_amdgcn_make_buffer_rsrc(ws, 0, WS_END, 0x00020000);

#define tg (xcd * 32 + idx)
#define ct ((xcd & 6) + ((idx >> 2) & 1))
#define mt (idx >> 3)
#define nt8 ((xcd & 1) * 4 + (idx & 3))
  const int gi = aux ? 0 : idx - 4;
  int ch0 = xcd * 80 + (aux ? 56 + 6 * idx : 2 * gi);
  u32x2 xr[5];
  unsigned pr[6], pn[6];
  auto ldpar = [&](unsigned (&o)[6], int pair) {
    const int e = (ch0 + 2 * pair + (w & 1)) * (NS / 2) + lane;
    o[0] = ldnt((const unsigned*)Are + e); o[1] = ldnt((const unsigned*)Aim + e); o[2] = ldnt((const unsigned*)Bre + e);
    o[3] = ldnt((const unsigned*)Bim + e); o[4] = ldnt((const unsigned*)Cre + e); o[5] = ldnt((const unsigned*)Cim + e);
  };
  if (w < 4) {
    const char* src = (const char*)x + (unsigned)((tg * 8 + 2 * w + (lane >> 5)) * D * 2 + (lane & 31) * 8);
#pragma unroll
    for (int i = 0; i < 5; ++i) xr[i] = *(const u32x2*)(src + i * 256);
  }
  if (w < 4 || aux) ldpar(pr, w >> 2);
  if (w >= 4 && aux) ldpar(pn, 2);
  LGKM_BAR();
  int m = (xcd >> 1) * 4 + gi / 7, n = (xcd & 1) * 7 + gi % 7;
  const int lane_ = lane, w_ = w, xcd_ = xcd, idx_ = idx, m_ = m, n_ = n, ch0_ = ch0;
  unsigned vx[8], vw[8];
  auto stage_setup = [&]() {
#pragma unroll
    for (int j = 0; j < 8; ++j) {
      vx[j] = (8 * j + (lane >> 3)) * (D * 2) + (((lane & 7) ^ (4 * (j & 1) + q)) << 4);
      int c = i16 ^ (q << 2);
      if (n == 13) c &= 7;
      vw[j] = (4 * j + q) * (F * 2) + c * 16;
    }
  };
  auto stage = [&](int kt0, int slot, bool thr = false) {
    int kt = kt0 + (ROT3 ? ((n >= 7 ? n - 7 : n) + 3 * (m & 3)) : 0);
    if (kt >= 20) kt -= 20; else if (kt >= 10) kt -= 10;
    const int lw = w - 4;
    const char* xb = (const char*)x + (unsigned)((m * 128 + 64 * lw) * D * 2 + kt * 128);
    const char* wb = (const char*)w1 + (unsigned)((kt * 64 + 32 * lw) * F * 2 + n * 256);
    const unsigned sb = lds0 + slot * 32768 + lw * 8192;
    if (thr) asm volatile("s_waitcnt vmcnt(8)" ::: "memory");
    glds16x8(xb, vx, sb);
    if (thr) asm volatile("s_waitcnt vmcnt(8)" ::: "memory");
    glds16x8(wb, vw, sb + 16384);
  };
  unsigned tag, sdt[3], sdk[3];
  auto scalars = [&]() {
    asm volatile("s_load_dword %0, %7, 0x0\n\ts_load_dword %1, %8, 0x0\n\ts_load_dword %2, %8, 0x4\n\ts_load_dword %3, %8, 0x8\n\t"
                 "s_load_dword %4, %9, 0x0\n\ts_load_dword %5, %9, 0x4\n\ts_load_dword %6, %9, 0x8\n\ts_waitcnt lgkmcnt(0)"
                 : "=&s"(tag), "=&s"(sdt[0]), "=&s"(sdt[1]), "=&s"(sdt[2]), "=&s"(sdk[0]), "=&s"(sdk[1]), "=&s"(sdk[2])
                 : "s"(ws + OFF_EPOCH), "s"(logdt + ch0), "s"(Dsk + ch0) : "memory");
    tag += 1u;
  };
  const int stg = aux ? 0 : L_STG;
  struct KS { lptr a0, a1, b; u32x4 fa0, fa1, fb; f32x4 k0, k1; };
  auto k_init = [&](KS& k, int tab) {
    const int r0 = i16 < 8 ? i16 : 8 + (int)((0x43222100u >> (4 * (i16 - 8))) & 15u);
    const int r1 = i16 < 8 ? 13 + 3 * (i16 >> 2) + min(i16 & 3, 2) : 11 + i16;
    const int r2 = 28 + ((w >> 1) & 1) * 16 + i16;
    lptr t = lds + tab + (w & 1) * KG_CH + q * 32;
    k.a0 = t + (r0 >> 1) * KG_ROW + (r0 & 1) * 8; k.a1 = t + (r1 >> 1) * KG_ROW + (r1 & 1) * 8; k.b = t + (r2 >> 1) * KG_ROW + (r2 & 1) * 8;
    k.k0 = (f32x4){}; k.k1 = (f32x4){};
  };
  auto k_read = [&](KS& k, int off) {
    auto rd2 = [&](lptr a) { const u32x2 u = *(LDSP u32x2*)a, v = *(LDSP u32x2*)(a + 16); return (u32x4){u[0], u[1], v[0], v[1]}; };
    k.fa0 = rd2(k.a0 + 2 * off); k.fa1 = rd2(k.a1 + 2 * off); k.fb = rd2(k.b + 2 * off);
  };
  auto k_mma = [&](KS& k) { k.k0 = mfma16(k.fa0, k.fb, k.k0); k.k1 = mfma16(k.fa1, k.fb, k.k1); };
  struct KO { u32x2 o0; u32x4 o1, o2; };
  auto k_pack = [&](KS& k, unsigned dk, KO& o) {
    if (lane == 0 && !(w & 2)) k.k0[0] += (w & 1) ? bfhi(dk) : bflo(dk);
    const bool lo = q < 2, odd = lane & 1;
    const float a = k.k1[0], b = k.k1[1], c = k.k1[2], d = k.k1[3], e3 = lo ? c : b;
    o.o0 = (u32x2){pk2(k.k0[0], k.k0[1]), pk2(k.k0[2], k.k0[3])};
    o.o1 = (u32x4){pk2(a, a), pk2(a, lo ? b : a), pk2((lo || odd) ? b : a, b), pk2(e3, e3)};
    o.o2 = (u32x4){pk2(c, c), pk2(c, c), pk2(odd ? d : c, d), pk2(d, d)};
    if (lo) o.o2 = (u32x4){};
    PIN(o.o0); PIN(o.o1); PIN(o.o2);
  };
  auto k_write = [&](const KO& o, int img) {
    lptr ib = lds + img + (w & 1) * KIMG_SZ, kd = ib + 32 + (((w >> 1) & 1) * 16 + i16) * 16;
    *(LDSP u32x2*)(kd + (q >> 1) * 512 + (q & 1) * 8) = o.o0;
    *(LDSP u32x4*)(kd + (q < 2 ? 1024 + q * 512 : q * 1024)) = o.o1;
    lptr pad = q == 0 ? lds + img + 2 * KIMG_SZ + (w & 1) * UIMG_SZ + i16 * 16 : ib + (lane & 1) * 16;
    *(LDSP u32x4*)(q < 2 ? pad : kd + q * 1024 + 512) = o.o2;
  };
  auto u_load = [&](u32x4 (&ug)[4], int pair, int hf) {
#pragma unroll
    for (int k = 0; k < 4; ++k)
      ug[k] = __builtin_amdgcn_raw_buffer_load_b128(rws, (lane >> 1) * 10240 + (lane & 1) * 16,
                                                    OFF_XT + (hf * 128 + k * 32) * 10240 + (ch0 + 2 * pair) * 16, SC1);
  };
  auto u_write = [&](const u32x4 (&ug)[4], int img, int hf) {
#pragma unroll
    for (int k = 0; k < 4; ++k)
      *(LDSP u32x4*)(lds + img + 2 * KIMG_SZ + (lane & 1) * UIMG_SZ + 256 + (hf * 128 + k * 32 + (lane >> 1)) * 16) = ug[k];
  };
  auto w2_off = [&]() {
    const unsigned u = min(lane, KS2 - 1) * 4 + (nt8 >> 1);
    return (int)(OFF_FLN + ((u / 28) * 32 + 4 + u % 28) * 128 + 4);
  };

  auto xt_job = [&]() {
    lds_wait<1>(lds + F_LNC, 4u);
    u32x4 xt[10];
    lptr sx = lds + stg + qq * STG_ROW + q * 32 + p * 8;
#pragma unroll
    for (int t = 0; t < 10; ++t) xt[t] = trd2(sx + t * 128, sx + t * 128 + 4 * STG_ROW);
    const u32x4 ab8 = *(LDSP u32x4*)(lds + L_AB8 + p * 16);
#pragma unroll
    for (int t = 0; t < 10; ++t) st16(xt[t], rws, OFF_XT + tg * 10240 + t * 1024 + lane * 16);
    if (lane < 4) st16(ab8, rws, OFF_AB + tg * 64 + lane * 16);
    lds_arrive(lds + F_S2F, lane);
    STAMP(1);
  };

  auto ksof = [&](int i) {
    int ir = i;
    if (ROT6 && i < 13) { ir += (nt8 * 13 + 4) / 8; if (ir >= 13) ir -= 13; }
    return (w & 3) + 4 * ir;
  };
  struct WR { u32x4 v[8][5]; };
  auto w_load = [&](WR& wr, int s0, int s1) {
    const rsrc_t rw = __builtin_amdgcn_make_buffer_rsrc(ws + OFF_W2T + nt8 * KS2 * 5120, 0, KS2 * 5120, 0x00020000);
#pragma unroll
    for (int s = s0; s < s1; ++s) {
      const int so = ksof(s + 6) * 5120;
#pragma unroll
      for (int b = 0; b < 4; ++b) wr.v[s][b] = __builtin_amdgcn_raw_buffer_load_b128(rw, lane * 16 + b * 1024, so, SC1);
      wr.v[s][4] = __builtin_amdgcn_raw_buffer_load_b128(rw, lane * 16, so + 4096, SC1);
    }
  };
  auto w_serve = [&](WR& wr, auto head) {
    constexpr int S0 = decltype(head)::value;
    FRESH();
    const int hw = w - 4;
    auto dma = [&]() {
#pragma unroll 1
      for (int s = 0; s < 6; ++s) {
        if (S0 && s < NSLOW) DRAIN();
        else asm volatile("s_waitcnt vmcnt(%0)" :: "i"(THR) : "memory");
        glds5k(ws + (unsigned)(OFF_W2T + (nt8 * KS2 + ksof(s)) * 5120), lane * 16, lds0 + hw * W2L_WAVE + s * 5120);
      }
    };
    const bool late = !S0 && hw < 2;
    if (!late) dma();
#pragma unroll
    for (int s = S0; s < 8; ++s) {
      asm volatile("s_waitcnt vmcnt(%0)" :: "i"(THR) : "memory");
      if (s == S0 + 2 && !late) { asm volatile("s_waitcnt vmcnt(10)" ::: "memory"); lds_set(lds + F_WF + hw * 4, 6u); STAMP(4); }
      w_load(wr, s, s + 1); SBAR();
    }
    if (late) {
      lds_wait<4>(lds + F_PASS, hw ? 4u : 8u);
      dma();
      DRAIN();
      lds_set(lds + F_WF + hw * 4, 6u);
    }
    STAMP(5);
#pragma unroll
    for (int s = 0; s < 8; ++s) {
      if (s == 7 && hw >= 2) break;
      lds_wait<1>(lds + F_GP + hw * 4, s + 1);
#pragma unroll
      for (int b = 0; b < 5; ++b) *(LDSP u32x4*)(lds + hw * W2L_WAVE + (s % 6) * 5120 + b * 1024 + lane * 16) = wr.v[s][b];
      lds_set(lds + F_WF + hw * 4, s + 7);
    }
    STAMP(6);
  };

#define HBAR() do { SBAR(); LGKM_BAR(); SBAR(); } while (0)
  if (!aux && w >= 4) {
    WR wr;
    if (w < 6) {
      __builtin_amdgcn_s_setprio(3);
      stage_setup();
      if (LSLEEP) __builtin_amdgcn_s_sleep(LSLEEP);
      stage(0, 0);
      scalars();
      stage(1, 1, true);
      STAMP(11);
      lds_wait<1>(lds + F_S2F, 2u);
      STAMP(1);
      stage(2, 2);
      STAMP(12);
      KS ks;
      KO ko;
      k_init(ks, L_KG);
      WAIT_BAR(32);
      k_read(ks, 0);
      WAIT_BAR(16);
      int slot = 0;
#pragma unroll 1
      for (int kt = 3; kt < 10; ++kt) {
        stage(kt, slot);
        slot = slot == 2 ? 0 : slot + 1;
        k_mma(ks); k_read(ks, (kt - 2) * 64);
        WAIT_BAR(16);
      }
      w_load(wr, 0, 2); SBAR();
      k_mma(ks); k_pack(ks, sdk[0], ko);
      WAIT_BAR(10);
      k_write(ko, L_IMG);
      w_load(wr, 2, 3); SBAR();
      LGKM_BAR();
      w_load(wr, 3, 4); SBAR();
      __builtin_amdgcn_s_setprio(0);
      STAMP(3);
    } else {
      u32x4 sj[10];
      const int u = min(xcd * 28 + idx - 4, 215);
      if (w == 6) {
        const rsrc_t r1 = __builtin_amdgcn_make_buffer_rsrc((void*)w1, 0, D * F * 2, 0x00020000);
        int vo = (40 * m + q) * F * 2 + n * 256 + (n == 13 ? i16 & 7 : i16) * 16;
#pragma unroll
        for (int k = 0; k < 10; ++k) sj[k] = __builtin_amdgcn_raw_buffer_load_b128(r1, vo, k * 4 * F * 2, 0);
      } else {
        const char* src = (const char*)w2 + (unsigned)((u >> 2) * 32 * D * 2 + (u & 3) * 320);
        unsigned vo[5];
#pragma unroll
        for (int k = 0; k < 5; ++k) { const unsigned id = k * 64 + lane; vo[k] = (id / 20) * (D * 2) + (id % 20) * 16; }
#pragma unroll
        for (int k = 0; k < 10; ++k) glds16nt(src + (k / 5) * 16 * D * 2, vo[k % 5], lds0 + L_W2S + k * 1024);
      }
      scalars();
      if (w == 6) {
        xt_job();
      }
      auto colsum = [&]() {
        const unsigned one = (lane & 1) ? 0x3f800000u : 0x00003f80u;
        const u32x4 sel = {i16 >> 1 == 0 ? one : 0u, i16 >> 1 == 1 ? one : 0u, i16 >> 1 == 2 ? one : 0u, i16 >> 1 == 3 ? one : 0u};
        f32x4 cs = {};
#pragma unroll
        for (int k = 0; k < 10; ++k) cs = mfma16(sel, sj[k], cs);
        if (lane < 32) st16(__builtin_bit_cast(u32x4, cs), rws, OFF_CS + m * CS_STRIDE + n * 512 + i16 * 32 + q * 16);
      };
      if (w == 7) {
        DRAIN();
        lptr sw = lds + L_W2S + (16 * (q >> 1) + 4 * (q & 1) + qq) * 320 + 8 * p;
#pragma unroll
        for (int k = 0; k < 10; ++k) sj[k] = trd2(sw + k * 32, sw + k * 32 + 8 * 320);
#pragma unroll
        for (int k = 0; k < (NOW2T ? 0 : 10); ++k)
          st16(sj[k], rws, OFF_W2T + ((((u & 3) * 2 + k / 5) * KS2 + (u >> 2)) * 5 + k % 5) * 1024 + lane * 16);
        lds_arrive(lds + F_S2F, lane);
      }
      __builtin_amdgcn_s_setprio(2);
      const int allo = OFF_ALL + (xcd * 4 + (idx & 3)) * 128;
      int fo = 0;
      unsigned f0 = 0, f1 = 0;
      bool todo = true;
      auto flag_if = [&](bool force, int word) {
        if (todo && (force || vm_idle())) {
          DRAIN();
          if (lane == 0) flag_store(rws, OFF_FLN + tg * 128 + word * 4, tag);
          todo = false;
          if (word < 2) STAMP(14);
        }
      };
      KS ks;
      KO ko;
      k_init(ks, L_KG);
      STAMP(12);
      int word = w - 6;
      auto side_step = [&](bool force) {
        flag_if(force, word);
        if (!todo && word == 0) { colsum(); word = 2; todo = true; }
      };
#pragma unroll 1
      for (int j = -48; j <= 4; ++j) {
        if (j < 0) {
          if (__all(*(volatile LDSP unsigned*)(lds + F_PQ) != 0)) { j = -1; continue; }
          __builtin_amdgcn_s_sleep(1);
        } else {
          HBAR();
        }
        side_step(j == 4 || (j == 1 && word == 0) || (j == 3 && word == 2));
        if (j > 0) k_mma(ks);
        if (j >= 0) k_read(ks, j * 64);
      }
      {
        const int t2 = ((i16 >> 2) * 2 + (n >= 7)) * 32 + 4 + (i16 & 3) * 7 + (n >= 7 ? n - 7 : n);
        fo = w == 6 ? OFF_FLN + ((lane & 16) ? t2 * 128 + 8 : (m * 16 + i16) * 128) : w2_off();
      }
      if (w == 6) {
        f0 = __builtin_amdgcn_raw_buffer_load_b32(rws, fo, 0, SC1);
        HBAR();
        k_mma(ks); k_read(ks, 5 * 64);
        HBAR();
        if (!__all(f0 == tag)) poll_words<2>(rws, fo, -1, tag);
        CFENCE();
        STAMP(13);
        f32x4 cp[8];
        u32x4 ua[4], ub[4];
        const u32x4 sx = __builtin_amdgcn_raw_buffer_load_b128(rws, lane * 16, OFF_AB + m * 1024, SC1);
#pragma unroll
        for (int k = 0; k < 8; ++k)
          cp[k] = __builtin_bit_cast(f32x4, __builtin_amdgcn_raw_buffer_load_b128(rws, (lane >> 5) * CS_STRIDE + (lane & 31) * 16,
                                                                                   OFF_CS + 2 * k * CS_STRIDE + n * 512, SC1));
        unsigned bb = *(const unsigned*)(b1 + min(n * 128 + 2 * lane, F - 2));
        k_mma(ks); k_read(ks, 6 * 64);
        HBAR();
        f1 = __builtin_amdgcn_raw_buffer_load_b32(rws, allo, 0, SC1);
        k_mma(ks); k_read(ks, 7 * 64);
        HBAR();
        k_mma(ks);
        float c[4];
#pragma unroll
        for (int e = 0; e < 4; ++e) {
          c[e] = cp[0][e];
#pragma unroll
          for (int k = 1; k < 8; ++k) { c[e] += cp[k][e]; PIN(c[e]); }
          const auto r = __builtin_amdgcn_permlane32_swap(__float_as_uint(c[e]), __float_as_uint(c[e]), false, false);
          c[e] = __uint_as_float(r[0]) + __uint_as_float(r[1]);
          PIN(c[e]);
        }
        k_pack(ks, sdk[0], ko);
        PIN(bb);
        HBAR();
        *(LDSP u32x4*)(lds + L_AB + lane * 16) = sx;
        if (lane < 32) *(LDSP f32x4*)(lds + L_CS + lane * 16) = (f32x4){c[0], c[1], c[2], c[3]};
        *(LDSP f32x2*)(lds + L_B1 + lane * 8) = (f32x2){bflo(bb), bfhi(bb)};
        k_write(ko, L_IMG);
        const bool early = __all(f1 == tag);
        CFENCE();
        if (early) { u_load(ua, 0, 0); u_load(ub, 0, 1); }
        HBAR();
        STAMP(3);
        if (!early) {
          poll_words<4>(rws, allo, -1, tag);
          u_load(ua, 0, 0); u_load(ub, 0, 1);
        }
        u_write(ua, L_IMG, 0); u_write(ub, L_IMG, 1);
        lds_set(lds + F_RDY, 1u);
        w_load(wr, 0, 4); SBAR();
      } else {
        HBAR();
        k_mma(ks); k_read(ks, 5 * 64);
        HBAR();
        f0 = __builtin_amdgcn_raw_buffer_load_b32(rws, fo, 0, SC1);
        k_mma(ks); k_read(ks, 6 * 64);
        HBAR();
        k_mma(ks); k_read(ks, 7 * 64);
        if (!__all(f0 == tag)) poll_words<2>(rws, fo, -1, tag);
        CFENCE();
        STAMP(13);
        HBAR();
        w_load(wr, 0, 2); SBAR();
        k_mma(ks); k_pack(ks, sdk[0], ko);
        HBAR();
        k_write(ko, L_IMG);
        w_load(wr, 2, 3); SBAR();
        HBAR();
        w_load(wr, 3, 4); SBAR();
        STAMP(3);
      }
      __builtin_amdgcn_s_setprio(0);
      STAMP(15);
    }
    w_serve(wr, IC<4>{});
  } else {
  scalars();
  if (w < 4) {
    f32x2 v[10];
#pragma unroll
    for (int i = 0; i < 10; ++i) v[i] = (f32x2){bflo(xr[i >> 1][i & 1]), bfhi(xr[i >> 1][i & 1])};
    STAMP(11);
    const unsigned e0 = __builtin_amdgcn_readlane(xr[0][0], 0), e1 = __builtin_amdgcn_readlane(xr[0][0], 32);
    const float x0 = bflo(lane < 32 ? e0 : e1);
    f32x2 s = {}, sq = {};
#pragma unroll
    for (int i = 0; i < 10; ++i) { const f32x2 d = v[i] - x0; s += d; sq = __builtin_elementwise_fma(d, d, sq); }
    float a = s[0] + s[1], b = sq[0] + sq[1];
    half_sum2(a, b);
    a *= (1.0f / D);
    const float var = fmaxf(b * (1.0f / D) - a * a, 0.f);
    const float mb = rbf(a + x0);
    const float r1 = __builtin_amdgcn_rcpf(rbf(__builtin_amdgcn_sqrtf(rbf(rbf(var) + 1e-5f))));
    lptr dst = lds + stg + (2 * w + (lane >> 5)) * STG_ROW + (lane & 31) * 8;
#pragma unroll
    for (int i = 0; i < 5; ++i) {
      const unsigned t0 = pkv(v[2 * i] - mb), t1 = pkv(v[2 * i + 1] - mb);
      *(LDSP u32x2*)(dst + i * 256) = (u32x2){pkv((f32x2){bflo(t0), bfhi(t0)} * r1), pkv((f32x2){bflo(t1), bfhi(t1)} * r1)};
    }
    const float m2 = rbf(((x0 - mb) + a) * r1);
    const float r2 = __builtin_amdgcn_rcpf(rbf(__builtin_amdgcn_sqrtf(rbf(rbf(var * r1 * r1) + 1e-5f))));
    if ((lane & 31) == 0) *(LDSP f32x2*)(lds + L_AB8 + (2 * w + (lane >> 5)) * 8) = (f32x2){r1 * r2, -(mb * r1 + m2) * r2};
    lds_arrive(lds + F_LNC, lane);
    STAMP(1);
  }
  if (w == 6) { PIN(pr[0]); PIN(pr[1]); PIN(pr[2]); PIN(pr[3]); PIN(pr[4]); PIN(pr[5]); xt_job(); }
  {
#pragma unroll 1
    for (int round = 0;; ++round) {
      FRESH();
      {
        auto par = [&](int k) { return (f32x2){bflo(pr[k]), bfhi(pr[k])}; };
        const unsigned sd = round ? sdt[2] : (w < 4 ? sdt[0] : sdt[1]);
        const float dt = rbf(__builtin_amdgcn_exp2f(((w & 1) ? bfhi(sd) : bflo(sd)) * 1.4426950408889634f));
        const f32x2 a2 = (par(0) * dt) * 1.4426950408889634f;
        const f32x2 rev = (par(1) * dt) * 0.15915494309189535f;
        lptr wr = lds + (w < 4 ? L_KG : L_KG1) + (w & 1) * KG_CH + lane * 16;
        auto put = [&](int pair, c2 u, c2 v) { *(LDSP u32x4*)(wr + pair * KG_ROW) = (u32x4){pkv(u.r), pkv(u.i), pkv(v.r), pkv(v.i)}; };
        if (((w >> 1) & 1) == 0) {
          const c2 z1 = cexpl(a2, -rev, 1.f), z256 = cexpl(a2, -rev, 256.f);
          c2 pk[9];
          pk[0] = cmul({par(4), -par(5)}, {par(2) * dt, par(3) * -dt});
#pragma unroll
          for (int k = 1; k <= 8; ++k) { pk[k] = cmul(pk[k - 1], z1); if (k & 1) put(k >> 1, pk[k - 1], pk[k]); }
          c2 c0 = cmul(pk[0], z256), c4 = cmul(pk[4], z256), c8 = cmul(pk[8], z256);
          put(4, c0, cmul(pk[2], z256)); put(5, c4, cmul(pk[6], z256));
          c0 = cmul(c0, z256); put(6, c8, c0);
          c4 = cmul(c4, z256); c8 = cmul(c8, z256); put(7, c4, c8);
          c0 = cmul(c0, z256); c4 = cmul(c4, z256); put(8, c0, c4);
          if (w == 0) lds_set(lds + F_PQ, 1u);
#pragma unroll
          for (int s2 = 0; s2 < 4; ++s2) { c8 = cmul(c8, z256); c0 = cmul(c0, z256); put(9 + s2, c8, c0); }
          c8 = cmul(c8, z256); put(13, c8, c8);
        } else {
          c2 q1 = cexpl(a2, rev, 8.f);
          const c2 z8 = q1;
          put(14, {(f32x2){1.f, 1.f}, (f32x2){0.f, 0.f}}, q1);
#pragma unroll
          for (int j = 1; j < 16; ++j) { const c2 q0 = cmul(q1, z8); q1 = cmul(q0, z8); put(14 + j, q0, q1); }
        }
      }
      STAMP(12);
      if (!aux) break;
      if (round == 0 && w == 6) { DRAIN(); if (lane == 0) flag_store(rws, OFF_FLN + tg * 128, tag); }
      {
        KS ks;
        KO ko;
        k_init(ks, w >= 4 ? L_KG1 : L_KG);
        if (round == 0) LGKM_BAR(); else { lds_arrive(lds + F_AX, lane); lds_wait<1>(lds + F_AX, 4u); }
        u32x4 a0[8], a1[8], bq[8];
#pragma unroll
        for (int j = 0; j < 8; ++j) { k_read(ks, j * 64); a0[j] = ks.fa0; a1[j] = ks.fa1; bq[j] = ks.fb; }
        if (round) lds_arrive(lds + F_AX, lane);
        SBAR();
#pragma unroll
        for (int j = 0; j < 8; ++j) { ks.fa0 = a0[j]; ks.fa1 = a1[j]; ks.fb = bq[j]; k_mma(ks); }
        k_pack(ks, round ? sdk[2] : (w < 4 ? sdk[0] : sdk[1]), ko);
        if (round == 0) LGKM_BAR();
        k_write(ko, round ? L_IMG : (w >> 2) * IMG_PAIR);
        lds_arrive(lds + (round ? F_RD2 : F_RDY), lane);
      }
      if (round == 1 || w < 4) break;
#pragma unroll
      for (int e = 0; e < 6; ++e) pr[e] = pn[e];
    }
  }
    if (aux) {
      if (w == 0) {
#pragma unroll 1
        for (int spin = 0; spin < SPIN_MAX; ++spin) {
          unsigned d = 0;
#pragma unroll
          for (int k = 0; k < 4; ++k) d |= __builtin_amdgcn_raw_buffer_load_b32(rws, (k * 64 + lane) * 128, OFF_FLN, SC1) ^ tag;
          if (__all(d == 0)) break;
          __builtin_amdgcn_s_sleep(2);
          CFENCE();
        }
        CFENCE();
        lds_set(lds + F_S2F, 2u);
        if (lane == 0) {
          flag_store(rws, OFF_ALL + (xcd * 4 + idx) * 128, tag);
          if (xcd == 0 && idx == 0) flag_store(rws, OFF_EPOCH, tag);
        }
        STAMP(13);
      }
      if (w < 6) {
        lds_wait<1>(lds + F_S2F, 2u);
        u32x4 ug[4];
        u_load(ug, w >> 1, w & 1);
        u_write(ug, w < 4 ? (w >> 1) * IMG_PAIR : L_IMG, w & 1);
        lds_arrive(lds + (w < 4 ? F_RDY : F_RD2), lane);
      }
    }
    if (w >= 4) {
      if (w == 7) { poll_words<6>(rws, w2_off(), -1, tag); lds_set(lds + F_W2OK, 1u); }
      lds_wait<2>(lds + F_W2OK, 1u);
      lds_wait<2>(lds + F_AX, 8u);
      WR wr;
      w_serve(wr, IC<0>{});
    }
  }
  FRESH();
  if (w < 4) {
    WAITCNT(0);
    if (!aux) {
    f32x16 acc[2][2];
    const int wf2 = w & 1, wt = w >> 1;
    const bool act = !(n == 13 && wf2);
    int xo[4], wo[2];
    {
      const int r = lane & 31, h = lane >> 5;
#pragma unroll
      for (int ks = 0; ks < 4; ++ks) { xo[ks] = (64 * wt + r) * 128 + (((2 * ks + h) ^ ((r >> 1) & 7)) << 4); PIN(xo[ks]); }
#pragma unroll
      for (int ft = 0; ft < 2; ++ft) { wo[ft] = (8 * h + qq) * 256 + (((2 * wf2 + ft) ^ qq) << 6) + 32 * (q & 1) + 8 * p; PIN(wo[ft]); }
    }
    struct Frags { u32x2 a[2][2][2]; u32x4 b[2][2]; };
    Frags f0, f1;
    lptr xp[4], wp[2];
#pragma unroll
    for (int j = 0; j < 4; ++j) xp[j] = lds + xo[j];
    wp[0] = lds + wo[0]; wp[1] = lds + wo[1];
    auto rd = [&](Frags& f, int hs, int i) {
      const int k = i / 6, e = i % 6, ks = 2 * hs + k;
      if (e < 2) f.a[k][0][e] = trd(wp[0] + 16384 + ks * 4096 + e * 1024);
      else if (e < 4) f.b[k][e - 2] = *(LDSP u32x4*)(xp[ks] + (e - 2) * 4096);
      else f.a[k][1][e - 4] = trd(wp[1] + 16384 + ks * 4096 + (e - 4) * 1024);
    };
    auto half_step = [&](Frags& cur, const Frags& prv, int hs, int mode, int nslot) {
#pragma unroll
      for (int i = 0; i < 8; ++i) {
        const int k = i >> 2, a = (i >> 1) & 1, b = i & 1;
        acc[a][b] = mfma32((u32x4){prv.a[k][a][0][0], prv.a[k][a][0][1], prv.a[k][a][1][0], prv.a[k][a][1][1]}, prv.b[k][b], acc[a][b]);
        SBAR();
        if (mode == 0 && i < 6) { rd(cur, hs, 2 * i); rd(cur, hs, 2 * i + 1); }
        if (mode == 1) {
          if (i < 4) { rd(cur, hs, 3 * i); rd(cur, hs, 3 * i + 1); rd(cur, hs, 3 * i + 2); }
          if (i == 4) { xp[0] = lds + nslot + xo[0]; xp[1] = lds + nslot + xo[1]; PIN(xp[0]); PIN(xp[1]); }
          if (i == 5) { xp[2] = lds + nslot + xo[2]; xp[3] = lds + nslot + xo[3]; PIN(xp[2]); PIN(xp[3]); }
          if (i == 6) { wp[0] = lds + nslot + wo[0]; wp[1] = lds + nslot + wo[1]; PIN(wp[0]); PIN(wp[1]); }
          if (i == 7) LGKM_BAR();
        }
        SBAR();
      }
    };
    WAIT_BAR(0);
    if (act) {
      __builtin_amdgcn_s_setprio(1);
#pragma unroll
      for (int i = 0; i < 12; ++i) { rd(f0, 0, i); SBAR(); }
#pragma unroll
      for (int a = 0; a < 2; ++a)
#pragma unroll
        for (int b = 0; b < 2; ++b) { acc[a][b] = (f32x16){}; PIN(acc[a][b]); }
      int slot = 0;
#pragma unroll 1
      for (int kt = 0; kt < 9; ++kt) {
        slot = slot == 65536 ? 0 : slot + 32768;
        half_step(f1, f0, 1, 1, slot);
        half_step(f0, f1, 0, 0, 0);
      }
      half_step(f1, f0, 1, 1, 0);
      half_step(f0, f1, 0, 3, 0);
    } else {
#pragma unroll 1
      for (int kt = 0; kt < 10; ++kt) LGKM_BAR();
    }
    STAMP(3);
    FRESH();
    if (act) {
      const int h = lane >> 5;
      f32x4 c4[2][4], b4[2][4];
      f32x2 abv[2];
#pragma unroll
      for (int tt = 0; tt < 2; ++tt) abv[tt] = *(LDSP f32x2*)(lds + L_AB + (64 * wt + 32 * tt + (lane & 31)) * 8);
#pragma unroll
      for (int ft = 0; ft < 2; ++ft)
#pragma unroll
        for (int g = 0; g < 4; ++g) {
          c4[ft][g] = *(LDSP f32x4*)(lds + L_CS + (64 * wf2 + 32 * ft + 8 * g + 4 * h) * 4);
          b4[ft][g] = *(LDSP f32x4*)(lds + L_B1 + (64 * wf2 + 32 * ft + 8 * g + 4 * h) * 4);
        }
      constexpr float C0 = -2.0f * 0.7978845608028654f * 1.4426950408889634f, C1 = C0 * 0.044715f;
      f32x2 k0 = {C0, C0}, k1 = {C1, C1};
      PIN(k0); PIN(k1);
      const f32x4 K0 = {k0[0], k0[1], k0[0], k0[1]}, K1 = {k1[0], k1[1], k1[0], k1[1]};
      const int hbase = OFF_H + ((((m >> 1) * 4 + (q & 1)) * KS2 + 4 * n + 2 * wf2) * 4 + 2 * (m & 1) + wt) * 1024 + (h * 16 + i16) * 16;
      u32x4 hv[8];
      int ha[4];
#pragma unroll
      for (int e = 0; e < 4; ++e) { ha[e] = hbase + (2 * (e & 1) * KS2 + (e >> 1)) * 4096; PIN(ha[e]); }
      SBAR();
#pragma unroll
      for (int ft = 0; ft < 2; ++ft)
#pragma unroll
        for (int tt = 0; tt < 2; ++tt) {
          const f32x2 ab = abv[tt];
#pragma unroll
          for (int sg = 0; sg < 2; ++sg) {
            unsigned o[4];
#pragma unroll
            for (int e = 0; e < 2; ++e) {
              const int g4 = 2 * sg + e;
              const f32x4 av = {acc[ft][tt][4 * g4], acc[ft][tt][4 * g4 + 1], acc[ft][tt][4 * g4 + 2], acc[ft][tt][4 * g4 + 3]};
              const f32x4 z = ab[0] * av + (ab[1] * c4[ft][g4] + b4[ft][g4]);
              f32x4 u = z * ((z * z) * K1 + K0);
              PIN(u);
              f32x4 g;
#pragma unroll
              for (int r = 0; r < 4; ++r) g[r] = __builtin_amdgcn_exp2f(u[r]);
              g += 1.0f;
#pragma unroll
              for (int r = 0; r < 4; ++r) g[r] = __builtin_amdgcn_rcpf(g[r]);
              g *= z;
              o[2 * e] = pk2(g[0], g[1]); o[2 * e + 1] = pk2(g[2], g[3]);
            }
            hv[(ft * 2 + tt) * 2 + sg] = (u32x4){o[0], o[1], o[2], o[3]};
            st16(hv[(ft * 2 + tt) * 2 + sg], rws, ha[ft * 2 + tt] + sg * 512);
          }
        }
#pragma unroll
      for (int e = 0; e < 8; ++e) asm volatile("" :: "v"(hv[e]), "v"(ha[e >> 1]));
    }
    STAMP(4);
    }

  u32x2 sl[2];
#pragma unroll 1
  for (int pass = aux ? 0 : 2; pass < 3; ++pass) {
    FRESH();
    {
      const int hw = w;
      const int pair = aux ? (pass == 2 ? 2 : 1 - pass) : 0;
      const int ch = ch0 + 2 * pair + (hw & 1);
      const int part = ((hw >> 1) ^ pass) & 1;
      lptr ib = lds + (pass == 2 ? L_IMG : pair * IMG_PAIR);
      lptr ub = ib + 2 * KIMG_SZ + (hw & 1) * UIMG_SZ + 32 + 32 * p + 64 * q + 8 * qq;
      lptr kb = ib + (hw & 1) * KIMG_SZ + 32 + 224 - 32 + 8 * p - 64 * q - 8 * qq;
      auto ldU = [&](int e) { return trd2(ub + 128 * e, ub + 128 * e + 32); };
      auto ldK = [&](int g) { return trd2(kb + 256 * g + 32, kb + 256 * g); };
      const bool last = lane == 63;
      Carry cy = {0.f, 0.f, 0.f};
      const int so = (q * D + ch) * 128 + i16 * 8;
      auto hpublish = [&]() {
        if (lane == 0) {
          if (!aux) flag_store(rws, OFF_FH + ((m >> 1) * 32 + (m & 1) * 14 + n) * 128 + w * 4, tag);
          else if (pass) flag_store(rws, OFF_FS + ((part ^ 1) * D + ch0 + 2 * (2 - pass) + (hw & 1)) * 128, tag);
        }
        STAMP(5);
      };
      auto sstore = [&](const float* y, int cti) {
        __builtin_amdgcn_raw_buffer_store_b64((u32x2){pk2(y[0], y[1]), pk2(y[2], y[3])}, rws, so, OFF_SP + cti * (4 * D * 128), SC1);
      };
      lds_wait<0>(lds + ((aux && pass == 2) ? F_RD2 : F_RDY), aux ? (pass == 2 ? 6u : 12u) : 1u);
      STAMP(14);
      if (pass == 0) STAMP(3);
      if (part == 0) {
        u32x2 U[24][2], K[12][2];
        auto rU = [&](int e, int t) { U[e][t] = trd(ub + 128 * e + 32 * t); };
        auto rK = [&](int g, int t) { K[g][t] = trd(kb + 256 * g + 32 - 32 * t); };
        auto ld12 = [&](int c, int i) {
          if (i < 2) rK(2 * c, i); else if (i < 10) rU(4 * c + ((i - 2) >> 1), i & 1); else rK(2 * c + 1, i & 1);
        };
#pragma unroll
        for (int i = 0; i < 24; ++i) { ld12(i / 12, i % 12); SBAR(); }
        f32x4 zp[4];
        float pm[4], h2[4], h1[4], yo[4];
        unsigned yp[2];
        auto ror = [](float v) { return __int_as_float(__builtin_amdgcn_update_dpp(0, __float_as_int(v), 0x13C, 0xf, 0xf, true)); };
        auto post = [&](int c, int sidx) {
          const int r = sidx & 3;
          if (sidx < 4) { pm[r] = last ? (r ? zp[r - 1][3] : cy.p3) : zp[r][3]; PIN(pm[r]); }
          else if (sidx < 8) { h2[r] = zp[r][2] + ror(pm[r]); PIN(h2[r]); }
          else if (sidx < 12) { pm[r] = last ? (r ? h2[r - 1] : cy.p2) : h2[r]; PIN(pm[r]); }
          else if (sidx < 16) { h1[r] = zp[r][1] + ror(pm[r]); PIN(h1[r]); }
          else if (sidx < 20) { pm[r] = last ? (r ? h1[r - 1] : cy.p1) : h1[r]; PIN(pm[r]); }
          else if (sidx < 24) { yo[r] = zp[r][0] + ror(pm[r]); PIN(yo[r]); }
          else if (sidx < 26) { yp[sidx - 24] = pk2(yo[2 * (sidx - 24)], yo[2 * (sidx - 24) + 1]); PIN(yp[sidx - 24]); }
          else {
            __builtin_amdgcn_raw_buffer_store_b64((u32x2){yp[0], yp[1]}, rws, so, OFF_SP + c * (4 * D * 128), SC1);
            cy.p1 = h1[3]; cy.p2 = h2[3]; cy.p3 = zp[3][3];
          }
        };
#pragma unroll
        for (int cti = 0; cti < 6; ++cti) {
          f32x4 z[4] = {};
          const int nm = 8 * cti + 6, nl = cti < 4 ? 12 : 2, nf = nl + (cti ? 27 : 0);
          int im = 0;
#pragma unroll
          for (int g = 2 * cti + 1; g >= 0; --g)
#pragma unroll
            for (int r = 0; r < 4; ++r) {
              const int gg = cti ? g : 1 - g, e = 4 * cti + r - 2 * gg;
              if (e < 0) continue;
              z[r] = mfma16((u32x4){U[e][0][0], U[e][0][1], U[e][1][0], U[e][1][1]},
                            (u32x4){K[gg][0][0], K[gg][0][1], K[gg][1][0], K[gg][1][1]}, z[r]);
              SBAR();
#pragma unroll
              for (int f = im * nf / nm; f < (im + 1) * nf / nm; ++f) {
                if (f >= nl) post(cti - 1, f - nl); else if (cti < 4) ld12(cti + 2, f);
              }
              SBAR();
              ++im;
            }
#pragma unroll
          for (int r = 0; r < 4; ++r) zp[r] = z[r];
          if (cti == HPA) { asm volatile("s_waitcnt vmcnt(%0)" :: "i"(HPA) : "memory"); hpublish(); SBAR(); }
        }
        float y[4];
        conv_post<4>(zp, y, cy, last);
        sl[0] = sl[1] = (u32x2){pk2(y[0], y[1]), pk2(y[2], y[3])};
      } else {
        u32x4 U[32], K[16];
        f32x4 z[9] = {};
        K[0] = ldK(0);
#pragma unroll
        for (int e = 31; e >= 21; --e) U[e] = ldU(e);
        K[1] = ldK(1);
        SBAR();
#pragma unroll
        for (int g = 0; g < 16; ++g) {
          if (20 - 2 * g >= 0) U[20 - 2 * g] = ldU(20 - 2 * g);
          if (19 - 2 * g >= 0) U[19 - 2 * g] = ldU(19 - 2 * g);
          if (g + 2 < 16) K[g + 2] = ldK(g + 2);
#pragma unroll
          for (int c = 31; c >= 23; --c)
            if (c - 2 * g >= 0) z[c - 23] = mfma16(U[c - 2 * g], K[g], z[c - 23]);
#pragma unroll
          for (int i = 0; i < 9; ++i) { SGB(0x8, 1); SGB(0x100, 1); }
          SBAR();
          if (g == HPB) { DRAIN(); hpublish(); SBAR(); }
        }
        float y[9];
        conv_post<9>(z, y, cy, last);
        sl[0] = (u32x2){pk2(y[1], y[2]), pk2(y[3], y[4])}; sl[1] = (u32x2){pk2(y[5], y[6]), pk2(y[7], y[8])};
      }
      STAMP(2);
      if (pass < 2) {
        lds_arrive(lds + F_PASS, lane);
        __builtin_amdgcn_raw_buffer_store_b64(sl[0], rws, so, OFF_SP + (5 + part) * (4 * D * 128), SC1);
        __builtin_amdgcn_raw_buffer_store_b64(sl[1], rws, so, OFF_SP + (5 + 2 * part) * (4 * D * 128), SC1);
      }
    }
  }

    FRESH();
    {
    const rsrc_t rh = __builtin_amdgcn_make_buffer_rsrc(ws + OFF_H + (ct * 4 + mt) * KS2 * 4096, 0, KS2 * 4096, 0x00020000);
    const int vo = lane * 16;
    constexpr int HD = HDEP;
    u32x4 hb[HD][4], wl[5];
    f32x4 a2[4][5];
#pragma unroll
    for (int a = 0; a < 4; ++a)
#pragma unroll
      for (int b = 0; b < 5; ++b) a2[a][b] = (f32x4){};
    auto loadH = [&](int i, int a) { hb[i % HD][a] = __builtin_amdgcn_raw_buffer_load_b128(rh, vo + a * 1024, ksof(i) * 4096, SC1); };
    lptr wp = lds + w * W2L_WAVE + lane * 16, wfp = lds + F_WF + w * 4, gpp = lds + F_GP + w * 4;
    PIN(wfp); PIN(gpp);
    auto ldsW = [&](int i, int b) { wl[b] = *(LDSP u32x4*)(wp + (i % 6) * 5120 + b * 1024); };
    if (w == 2) {
#pragma unroll 1
      for (int spin = 0; spin < SPIN_MAX; ++spin) {
        const u32x4 f = __builtin_amdgcn_raw_buffer_load_b128(rws, (ct * 32 + min(lane, 27)) * 128, OFF_FH, SC1);
        if (__all(f[0] == tag && f[1] == tag && f[2] == tag && f[3] == tag)) break;
        __builtin_amdgcn_s_sleep(2);
        CFENCE();
      }
      CFENCE();
      lds_set(lds + F_HRDY, 1u);
    } else {
      lds_wait<1>(lds + F_HRDY, 1u);
    }
    STAMP(6);
#pragma unroll
    for (int i = 0; i < HD - 1; ++i)
#pragma unroll
      for (int a = 0; a < 4; ++a) loadH(i, a);
    {
      const int part = (w >> 1) & 1, so = (q * D + ch0 + (aux ? 4 : 0) + (w & 1)) * 128 + i16 * 8;
      __builtin_amdgcn_raw_buffer_store_b64(sl[0], rws, so, OFF_SP + (5 + part) * (4 * D * 128), SC1);
      __builtin_amdgcn_raw_buffer_store_b64(sl[1], rws, so, OFF_SP + (5 + 2 * part) * (4 * D * 128), SC1);
    }
    lds_wait<1>(wfp, 6u);
#pragma unroll
    for (int b = 0; b < 5; ++b) ldsW(0, b);
    lds_set(gpp, 1u);
    SBAR();
    unsigned wf = 0;
#pragma unroll
    for (int i = 0; i < 14; ++i) {
      if (i == 13 && w >= 2) break;
      if (i + 1 >= 6 && i + 1 < 14 && !__all(wf >= (unsigned)(i + 2))) {
        if (i + 1 < 13 || w < 2) lds_wait<0>(wfp, i + 2);
      }
      CFENCE();
#pragma unroll
      for (int b = 0; b < 5; ++b) {
#pragma unroll
        for (int a = 0; a < 4; ++a) {
          a2[a][b] = mfma16(wl[b], hb[i % HD][a], a2[a][b]);
          if (a == 1 && b >= 1 && i + HD - 1 < 14) { SBAR(); loadH(i + HD - 1, b - 1); SBAR(); }
        }
        SBAR();
        if (i + 1 < 14) ldsW(i + 1, b);
        if (b == 2 && i + 2 >= 6 && i + 2 < 14) wf = *(volatile LDSP unsigned*)wfp;
        if (b == 0 && i == HD - 1) {
          asm volatile("s_waitcnt vmcnt(%0)" :: "i"(4 * (HD - 2)) : "memory");
          if (lane == 0) flag_store(rws, OFF_FS + (((w >> 1) & 1) * D + ch0 + (aux ? 4 : 0) + (w & 1)) * 128, tag);
        }
        SBAR();
      }
      if (i + 2 <= 8) lds_set(gpp, i + 2);
      SBAR();
    }
    __builtin_amdgcn_s_setprio(0);
    STAMP(7);
#pragma unroll
    for (int a = 0; a < 4; ++a)
#pragma unroll
      for (int b = 0; b < 5; ++b) *(LDSP f32x4*)(lds + w * W2L_WAVE + (a * 5 + b) * 1024 + lane * 16) = a2[a][b];
    }
  }
  FRESH();
  if (w == 7) {
    const int fo = OFF_FS + ((ct >= 6 ? D : 0) + nt8 * 80) * 128;
    poll_words<8>(rws, fo + lane * 128, fo + (64 + (lane & 15)) * 128, tag);
    STAMP(7);
    u32x4 sv[10];
#pragma unroll
    for (int k = 0; k < 10; ++k)
      sv[k] = __builtin_amdgcn_raw_buffer_load_b128(rws, lane * 16, OFF_SP + ((ct * 4 + mt) * D + nt8 * 80) * 128 + k * 1024, SC1);
#pragma unroll
    for (int k = 0; k < 10; ++k) *(LDSP u32x4*)(lds + L_SS + k * 1024 + lane * 16) = sv[k];
  }
  const int rho = w & 3, nt0 = w < 4 ? 0 : 3, ntn = w < 4 ? 3 : 2;
  {
    const size_t orow = (size_t)(ct * 256 + rho * 64 + mt * 16 + i16) * D + nt8 * 80 + nt0 * 16 + 4 * q;
    u32x2 xv[3], bb[3];
#pragma unroll
    for (int t = 0; t < 3; ++t) {
      const int tc = t < ntn ? t : 0;
      xv[t] = *(const u32x2*)(x + orow + tc * 16);
      bb[t] = *(const u32x2*)(b2 + nt8 * 80 + (nt0 + tc) * 16 + 4 * q);
    }
    STAMP(8);
    LGKM_BAR();
    STAMP(9);
#pragma unroll
    for (int t = 0; t < 3; ++t) {
      if (t >= ntn) break;
      const int nt = nt0 + t;
      lptr pp = lds + (rho * 5 + nt) * 1024 + lane * 16;
      const f32x4 z = (*(LDSP f32x4*)pp + *(LDSP f32x4*)(pp + W2L_WAVE)) +
                      (*(LDSP f32x4*)(pp + 2 * W2L_WAVE) + *(LDSP f32x4*)(pp + 3 * W2L_WAVE));
      lptr sp = lds + L_SS + (16 * nt + 4 * q) * 128 + i16 * 8 + rho * 2;
      const float bs[4] = {bflo(bb[t][0]), bfhi(bb[t][0]), bflo(bb[t][1]), bfhi(bb[t][1])};
      const float xs[4] = {bflo(xv[t][0]), bfhi(xv[t][0]), bflo(xv[t][1]), bfhi(xv[t][1])};
      float o[4];
#pragma unroll
      for (int e = 0; e < 4; ++e) {
        const float zz = z[e] + bs[e];
        const float g = zz * __builtin_amdgcn_rcpf(1.0f + __builtin_amdgcn_exp2f(zz * -1.4426950408889634f));
        o[e] = __builtin_fmaf(bflo(*(LDSP u16*)(sp + e * 128)), g, xs[e]);
      }
      *(u32x2*)(out + orow + t * 16) = (u32x2){pk2(o[0], o[1]), pk2(o[2], o[3])};
    }
  }
  STAMP(10);
  asm volatile(".Lfused_end:");
}

#undef ct
#undef mt
#undef nt8
#undef tg
}

extern "C" void kernel_launch(void* const* d_in, const int* in_sizes, int n_in, void* d_out, int out_size, void* d_ws,
                              size_t ws_size, hipStream_t stream) {
  (void)in_sizes; (void)n_in; (void)out_size; (void)ws_size;
  (void)hipFuncSetAttribute((const void*)fused, hipFuncAttributeMaxDynamicSharedMemorySize, LDS_SZ);
  fused<<<256, 512, LDS_SZ, stream>>>((const u16*)d_in[0], (const u16*)d_in[1], (const u16*)d_in[2], (const u16*)d_in[3],
                                      (const u16*)d_in[4], (const u16*)d_in[5], (const u16*)d_in[6], (const u16*)d_in[7],
                                      (const u16*)d_in[8], (const u16*)d_in[10], (const u16*)d_in[11], (const u16*)d_in[12],
                                      (const u16*)d_in[13], (char*)d_ws, (u16*)d_out);
}
```
